```python
import torch

def _softmax(x, axis=-1):
    return torch.softmax(x, dim=axis)
def _gelu(x):
    return torch.nn.functional.gelu(x, approximate="tanh")
def _silu(x):
    return torch.nn.functional.silu(x)
def _sigmoid(x):
    return torch.sigmoid(x)
def _layernorm(x, axis=-1, eps=1e-5):
    m = x.mean(dim=axis, keepdim=True)
    v = x.var(dim=axis, keepdim=True, unbiased=False)
    return (x - m) / torch.sqrt(v + eps)
def _scatter_add(tgt, idx, src, axis=0):
    return tgt.clone().index_add_(axis, idx, src)
def _segment_sum(src, seg, n):
    out = torch.zeros((n,) + src.shape[1:], dtype=src.dtype)
    return out.index_add_(0, seg, src)
def _pad(x, pw):
    flat = tuple(v for pair in reversed(pw) for v in pair)
    return torch.nn.functional.pad(x, flat)

kernel_name = 'parallel_sum__sliding_local_dilated__mixer_channel_mlp__prompt'

def setup_inputs():

    _w = (lambda a: (lambda t: t.to(torch.bfloat16) if t.is_floating_point() else t)(torch.as_tensor(a)))
    def _A_setup():
        B, S, D = 1, 2048, 640
        return {
            "x": _w((lambda *s: torch.randn(*s, dtype=torch.bfloat16) * 0.05)(B, S, D)),
            "wqkv": _w((lambda *s: torch.randn(*s, dtype=torch.bfloat16) * 0.05)(D, 3 * D)),
            "wo": _w((lambda *s: torch.randn(*s, dtype=torch.bfloat16) * 0.05)(D, D)),
        }
    _A_in = _A_setup()

    def _B_setup():
        B, S, D, F = 1, 2048, 640, 1536
        return {
            "x": _w((lambda *s: torch.randn(*s, dtype=torch.bfloat16) * 0.05)(B, S, D)),
            "w1": _w((lambda *s: torch.randn(*s, dtype=torch.bfloat16) * 3.2)(D, F) * (D ** -0.5)),
            "b1": _w((lambda *s: torch.randn(*s, dtype=torch.bfloat16) * 1.6)(F)),
            "w2": _w((lambda *s: torch.randn(*s, dtype=torch.bfloat16) * 0.2)(F, D) * (F ** -0.5)),
            "b2": _w((lambda *s: torch.randn(*s, dtype=torch.bfloat16) * 0.4)(D)),
        }
    _B_in = _B_setup()

    return {
        "A_x": _A_in["x"],
        "A_wqkv": _A_in["wqkv"],
        "A_wo": _A_in["wo"],
        "B_x": _B_in["x"],
        "B_w1": _B_in["w1"],
        "B_b1": _B_in["b1"],
        "B_w2": _B_in["w2"],
        "B_b2": _B_in["b2"],
    }

def reference(A_x, A_wqkv, A_wo, B_x, B_w1, B_b1, B_w2, B_b2):
    def _A_ref(x, wqkv, wo):
        B, S, D = x.shape
        H = 32; Dh = D // H
        W, dil = 8, 2
        qkv = (lambda x, *d: x.permute(*d))((x @ wqkv).reshape(B, S, 3, H, Dh), 2, 0, 3, 1, 4)
        q, k, v = qkv[0], qkv[1], qkv[2]
        scale = Dh ** -0.5
        pos = torch.arange(S)
        off = torch.arange(W) * dil
        idx = pos[:, None] - off[None, :]
        mask = idx >= 0
        idx_c = torch.where(mask, idx, 0).reshape(-1)
        kg = k[:, :, idx_c, :].reshape(B, H, S, W, Dh)
        vg = v[:, :, idx_c, :].reshape(B, H, S, W, Dh)
        logits = torch.einsum("bhsd,bhswd->bhsw", q, kg) * scale
        logits = torch.where(mask[None, None, :, :], logits, -1e9)
        attn = _softmax(logits, axis=-1)
        o = torch.einsum("bhsw,bhswd->bhsd", attn, vg)
        return (lambda x, *d: x.permute(*d))(o, 0, 2, 1, 3).reshape(B, S, D) @ wo

    def _B_ref(x, w1, b1, w2, b2):
        y = _layernorm(x, axis=-1)
        h = _gelu(torch.einsum("bsd,df->bsf", y, w1) + b1)
        return torch.einsum("bsf,fd->bsd", h, w2) + b2

    x = A_x
    xn = _layernorm(x, axis=-1)
    return x + _A_ref(xn, A_wqkv, A_wo) + _B_ref(xn, B_w1, B_b1, B_w2, B_b2)
```

```cpp
#include <hip/hip_runtime.h>
#include <stdint.h>

typedef unsigned short u16;
typedef unsigned int u32;
using bf16x8 = __attribute__((ext_vector_type(8))) short;
using s16x4 = __attribute__((ext_vector_type(4))) short;
using f32x4 = __attribute__((ext_vector_type(4))) float;
using u32x4 = __attribute__((ext_vector_type(4))) unsigned;
using u32x2 = __attribute__((ext_vector_type(2))) unsigned;
using i32x4 = __attribute__((ext_vector_type(4))) int;
typedef __bf16 bf16x2_t __attribute__((ext_vector_type(2)));
typedef float f32x2 __attribute__((ext_vector_type(2)));
#define LDS_AS __attribute__((address_space(3)))
#define GLB_AS __attribute__((address_space(1)))

static constexpr int S = 2048, D = 640, F = 1536, NQKV = 1920;
static constexpr int RT_STRIDE = 272 * 256;
static constexpr u32 OH_BYTES = 128u * RT_STRIDE;
static constexpr u32 WS_SYNC = 9u << 20;
static constexpr u32 SY_EPOCH = 0, SY_FLAG = 256, SY_STAT = 1024;
static constexpr u32 WS_USED = WS_SYNC + 8 * 8192;
#define OUT_AUX 16
static constexpr int SPIN_MAX = 1 << 22;

struct Params {
  const u16 *x, *wqkv, *wo, *w1, *b1, *w2, *b2;
  u16* out;
  char* ws;
};

#define STAMPW(w, idx) do {} while (0)
#define SB() __builtin_amdgcn_sched_barrier(0)
#define DA6 2
#define DA4 3
#define THR 6
#define STR2(x) #x
#define STR(x) STR2(x)
#define THROTTLE "s_waitcnt vmcnt(" STR(THR) ")"

__device__ __forceinline__ u32 pack_bf16(float lo, float hi) {
  f32x2 v = {lo, hi};
  bf16x2_t b = __builtin_convertvector(v, bf16x2_t);
  return __builtin_bit_cast(u32, b);
}
__device__ __forceinline__ float bf_lo(u32 u) { return __uint_as_float(u << 16); }
__device__ __forceinline__ float bf_hi(u32 u) { return __uint_as_float(u & 0xffff0000u); }
__device__ __forceinline__ float dot2(u32 a, u32 b, float acc) {
  return __builtin_amdgcn_fdot2_f32_bf16(__builtin_bit_cast(bf16x2_t, a), __builtin_bit_cast(bf16x2_t, b), acc, false);
}
__device__ __forceinline__ i32x4 mk_rsrc(const void* p, u32 bytes) {
  i32x4 r;
  unsigned long long a = (unsigned long long)p;
  r.x = (int)(u32)a; r.y = (int)((u32)(a >> 32) & 0xffffu); r.z = (int)bytes; r.w = 0x00020000;
  return r;
}
__device__ __forceinline__ void dma16(i32x4 rsrc, u32 voff, u32 soff, u32 ldsaddr) {
  asm volatile("s_mov_b32 m0, %3\n\t" THROTTLE "\n\tbuffer_load_dwordx4 %0, %1, %2 offen lds" ::"v"(voff), "s"(rsrc), "s"(soff), "s"(ldsaddr) : "memory");
}
__device__ __forceinline__ void dma16_sc1(i32x4 rsrc, u32 voff, u32 soff, u32 ldsaddr) {
  asm volatile("s_mov_b32 m0, %3\n\t" THROTTLE "\n\tbuffer_load_dwordx4 %0, %1, %2 offen sc1 lds" ::"v"(voff), "s"(rsrc), "s"(soff), "s"(ldsaddr) : "memory");
}
__device__ __forceinline__ void dma16_masked(i32x4 rsrc, u32 voff, u32 soff, u32 ldsaddr, unsigned long long mask) {
  asm volatile("s_mov_b32 m0, %3\n\t" THROTTLE "\n\ts_mov_b64 exec, %4\n\tbuffer_load_dwordx4 %0, %1, %2 offen lds\n\ts_mov_b64 exec, -1" ::"v"(voff), "s"(rsrc), "s"(soff), "s"(ldsaddr), "s"(mask) : "memory");
}
template <int N> __device__ __forceinline__ void wait_vm() { asm volatile("s_waitcnt vmcnt(%0)" ::"i"(N) : "memory"); }
__device__ __forceinline__ void wait_lgkm0() { __builtin_amdgcn_s_waitcnt(0xC07F); asm volatile("" ::: "memory"); }
__device__ __forceinline__ void bar() { asm volatile("s_barrier" ::: "memory"); }
__device__ __forceinline__ bf16x8 lds_b128(u32 addr) { return *(const LDS_AS bf16x8*)(uintptr_t)addr; }
__device__ __forceinline__ f32x4 lds_f4(u32 addr) { return *(const LDS_AS f32x4*)(uintptr_t)addr; }
__device__ __forceinline__ void lds_st_f4(u32 addr, f32x4 v) { *(LDS_AS f32x4*)(uintptr_t)addr = v; }
__device__ __forceinline__ bf16x8 lds_tr(u32 addr0, u32 addr1) {
  s16x4 a = __builtin_amdgcn_ds_read_tr16_b64_v4i16((LDS_AS s16x4*)(uintptr_t)addr0);
  s16x4 b = __builtin_amdgcn_ds_read_tr16_b64_v4i16((LDS_AS s16x4*)(uintptr_t)addr1);
  return __builtin_shufflevector(a, b, 0, 1, 2, 3, 4, 5, 6, 7);
}
__device__ __forceinline__ f32x4 mfma(bf16x8 a, bf16x8 b, f32x4 c) { return __builtin_amdgcn_mfma_f32_16x16x32_bf16(a, b, c, 0, 0, 0); }
__device__ __forceinline__ u32 ld_agent(const char* p) { return __hip_atomic_load((const GLB_AS u32*)p, __ATOMIC_RELAXED, __HIP_MEMORY_SCOPE_AGENT); }
__device__ __forceinline__ void st_agent(char* p, u32 v) { __hip_atomic_store((GLB_AS u32*)p, v, __ATOMIC_RELAXED, __HIP_MEMORY_SCOPE_AGENT); }

static constexpr u32 QA_SLOT = 18432, QB_SLOT = 15360, QB_BASE = 3 * QA_SLOT;
static constexpr u32 WA_SLOT = 16384, WB_SLOT = 13312, WB_BASE = 3 * WA_SLOT;
static constexpr u32 S2B_BASE = 73728, S2B_SLOT = 16384;
static constexpr u32 L_CS = 155648;
static constexpr u32 L_RS = L_CS + 1024;
static constexpr u32 L_BIAS = L_RS + 1152;
static constexpr u32 L_JUNK = L_BIAS + 768;
static constexpr u32 L_CNT = L_JUNK + 1024;
static constexpr u32 L_TOTAL = L_CNT + 16;
static constexpr u32 QROW = 496;

struct Ids { int mt, g, sg, m2, wgi, xc, gi, rot1, roth, roto; bool isw; };

template <int V> struct CW;
template <> struct CW<0> { static constexpr int NC = 8, NW = 8, NH = 2, NA = 4, BROW = 480, NX = 5; static constexpr u32 ASLOT = QA_SLOT, BSLOT = QB_SLOT, BBASE = QB_BASE, NSB = 6; };
template <> struct CW<1> { static constexpr int NC = 7, NW = 9, NH = 6, NA = 4, BROW = 480, NX = 5; static constexpr u32 ASLOT = QA_SLOT, BSLOT = QB_SLOT, BBASE = QB_BASE, NSB = 6; };
template <> struct CW<2> { static constexpr int NC = 6, NW = 6, NH = 4, NA = 4, BROW = 416, NX = 4; static constexpr u32 ASLOT = WA_SLOT, BSLOT = WB_SLOT, BBASE = WB_BASE, NSB = 7; };
template <int V> __device__ __forceinline__ constexpr int hunit(int j) {
  return V == 0 ? (j < 2 ? j : -1) : V == 1 ? (j < 4 ? j : (j >= 7 ? j - 3 : -1)) : (j < 4 ? j : -1);
}
template <int V> __device__ __forceinline__ u32 waddr(int j, u32 wA, u32 wB, u32 wX) {
  using C = CW<V>;
  return j < C::NA ? wA + j * 32 : (j < C::NC ? wB + (j - C::NA) * 32 : wX + (j - C::NC) * 32);
}

template <int V>
__device__ __forceinline__ void kstep(f32x4 (&acc)[4][CW<V>::NC], f32x4 (&acch)[CW<V>::NH], bf16x8 (&xc)[CW<V>::NX], bf16x8 (&xn)[CW<V>::NX],
                                      bf16x8 (&w)[CW<V>::NW], u32 xo, u32 xh, u32 wA, u32 wB, u32 wX, u32 wlast) {
  using C = CW<V>;
  const short one = (short)0x3f80;
  const bf16x8 ones = {one, one, one, one, one, one, one, one};
#pragma unroll
  for (int j = 0; j < C::NW; ++j) {
    if (j < C::NC) {
#pragma unroll
      for (int i = 0; i < 4; ++i) {
        acc[i][j] = mfma(w[j], xc[i], acc[i][j]);
        if (i == 0 && j < C::NX) xn[j] = lds_b128(j < 4 ? xo + j * 2048 : xh);
        if (i == 2) {
          u32 a = (j == 0) ? wlast : waddr<V>(j - 1, wA, wB, wX);
          w[j == 0 ? C::NW - 1 : j - 1] = lds_tr(a, a + 16 * C::BROW);
        }
        SB();
      }
    }
    if (hunit<V>(j) >= 0) acch[hunit<V>(j) < 0 ? 0 : hunit<V>(j)] = mfma(w[j], V == 2 ? ones : xc[C::NX - 1], acch[hunit<V>(j) < 0 ? 0 : hunit<V>(j)]);
    if (j >= C::NC) { u32 a = waddr<V>(j - 1, wA, wB, wX); w[j - 1] = lds_tr(a, a + 16 * C::BROW); }
    SB();
  }
}

template <int V>
__device__ __forceinline__ void s1_compute(const Params& p, f32x4 (&acc)[4][CW<V>::NC], f32x4 (&acch)[CW<V>::NH], int mh, int lane, int cA, int cB, int cX) {
  using C = CW<V>;
  const int j16 = lane & 15, g4 = lane >> 4, q = (lane >> 2) & 3, pp = lane & 3;
  const int rt0 = (V == 2) ? 4 * mh : 1 + 4 * mh;
  const u32 xl = (u32)(j16 * 128 + ((g4 ^ ((j16 >> 1) & 7)) * 16));
  u32 xo0 = xl + rt0 * 2048, xo1 = (xl ^ 64u) + rt0 * 2048, xh0 = xl, xh1 = xl ^ 64u;
  const u32 wl = (u32)(C::BBASE + (4 * g4 + q) * C::BROW + pp * 8);
  u32 wA = wl + cA * 32, wB = wl + cB * 32, wX = wl + cX * 32;
  bf16x8 xa[C::NX], xb[C::NX], w[C::NW];
  bar();
  STAMPW(0, 2);
#pragma unroll
  for (int i = 0; i < C::NX; ++i) xa[i] = lds_b128(i < 4 ? xo0 + i * 2048 : xh0);
#pragma unroll
  for (int j = 0; j < C::NW; ++j) { u32 a = waddr<V>(j, wA, wB, wX); w[j] = lds_tr(a, a + 16 * C::BROW); }
  wait_lgkm0();
  int bs = 0, as = 0;
  u32 wlast;
  auto stepB = [&]() {
    u32 dlt = (bs == (int)C::NSB - 1) ? (u32)(-(int)((C::NSB - 1) * C::BSLOT)) : C::BSLOT;
    bs = (bs == (int)C::NSB - 1) ? 0 : bs + 1;
    wlast = waddr<V>(C::NW - 1, wA, wB, wX);
    wA += dlt; wB += dlt; if (V == 1) wX += dlt;
  };
#pragma unroll 1
  for (int it = 0; it < 10; ++it) {
    stepB();
    bar();
    kstep<V>(acc, acch, xa, xb, w, xo1, xh1, wA, wB, wX, wlast);
    u32 dA = (as == 2) ? (u32)(-(int)(2 * C::ASLOT)) : C::ASLOT;
    as = (as == 2) ? 0 : as + 1;
    xo0 += dA; xo1 += dA; if (V != 2) { xh0 += dA; xh1 += dA; }
    stepB();
    bar();
    kstep<V>(acc, acch, xb, xa, w, xo0, xh0, wA, wB, wX, wlast);
  }
  STAMPW(0, 3);
}

template <bool ISW>
__device__ __forceinline__ void s1_loader(const Params& p, const Ids& id, int lw, int lane) {
  constexpr int NA = ISW ? 4 : 5, NAR = ISW ? 16 : 18, NBR = ISW ? 13 : 15, NSB = ISW ? 7 : 6, WN = ISW ? 16 : 17;
  constexpr u32 ASLOT = ISW ? WA_SLOT : QA_SLOT, BSLOT = ISW ? WB_SLOT : QB_SLOT, BBASE = ISW ? WB_BASE : QB_BASE;
  constexpr u32 WROW = ISW ? 3072 : 3840;
  const i32x4 rsA = mk_rsrc(p.x, S * D * 2);
  const i32x4 rsB = ISW ? mk_rsrc(p.w1, D * F * 2) : mk_rsrc(p.wqkv, D * NQKV * 2);
  const int row0 = id.mt * 128 - (ISW ? 0 : 16);
  u32 va[NA], vb[4];
#pragma unroll
  for (int i = 0; i < NA; ++i) {
    int ii = lw + 4 * i;
    int r = ii * 8 + (lane >> 3), pos = lane & 7;
    int ch = pos ^ ((r >> 1) & 7);
    va[i] = (ii < NAR) ? (u32)((row0 + r) * 1280 + ch * 16) : (u32)(id.mt * 128 * 1280);
  }
  const unsigned long long m0mask = (!ISW && lw == 0) ? ~0xFFull : ~0ull;
  auto issueA = [&](int a, int slot) {
    u32 l = slot * ASLOT + lw * 1024;
    a += id.rot1; if (a >= 10) a -= 10;
    dma16_masked(rsA, va[0], a * 128, l, m0mask);
#pragma unroll
    for (int i = 1; i < NA; ++i) dma16(rsA, va[i], a * 128, (lw + 4 * i < NAR) ? l + i * 4096 : L_JUNK);
  };
  STAMPW(4, 1);
  issueA(0, 0);
  SB();
#pragma unroll
  for (int i = 0; i < 4; ++i) {
    int ii = lw + 4 * i;
    int idx = ii * 64 + lane;
    constexpr int CPR = ISW ? 26 : 30;
    int rho = idx / CPR, pos = idx - rho * CPR;
    int kap = 8 * ((rho >> 2) & 3) + (rho & 3) + 4 * ((rho >> 4) & 1);
    u32 v;
    if (ISW) { int pc = pos < 24 ? pos : 23; v = (u32)(kap * 3072 + id.g * 384 + pc * 16); }
    else { int seg = pos / 10, off = pos - seg * 10; v = (u32)(kap * 3840 + seg * 1280 + id.g * 160 + off * 16); }
    vb[i] = (ii < NBR) ? v : 0u;
  }
  auto issueB = [&](int t, int slot) {
    u32 l = BBASE + slot * BSLOT + lw * 1024;
    t += 2 * id.rot1; if (t >= 20) t -= 20;
#pragma unroll
    for (int i = 0; i < 4; ++i) dma16(rsB, vb[i], t * 32 * WROW, (lw + 4 * i < NBR) ? l + i * 4096 : L_JUNK);
  };
  issueB(0, 0); issueB(1, 1); issueA(1, 1); issueB(2, 2); issueB(3, 3); issueA(2, 2); issueB(4, 4); issueB(5, 5);
  if (ISW) issueB(6, 6);
  constexpr int TOT = 3 * NA + 4 * NSB;
  wait_vm<TOT - NA - 4>();
  bar();
  wait_vm<TOT - NA - 8>();
  bar();
  int bs = 0, as = 0;
#pragma unroll 1
  for (int t = 0; t < 19; ++t) {
    if (t >= 2 && !(t & 1) && t <= 14) { issueA(t / 2 + 2, as); as = (as == 2) ? 0 : as + 1; }
    if (t >= 1 && t + NSB - 1 <= 19) { issueB(t + NSB - 1, bs); bs = (bs == NSB - 1) ? 0 : bs + 1; }
    if (t < 13) wait_vm<WN>(); else wait_vm<0>();
    bar();
  }
}

__device__ __forceinline__ u32 produce_stats(const Params& p, const Ids& id, int cw, int lane) {
  const int n = (id.wgi < 4) ? 3 : 2;
  const u32 ep = ld_agent(p.ws + WS_SYNC + id.sg * 8192 + SY_EPOCH);
  u32 u[3][5];
#pragma unroll
  for (int i = 0; i < 3; ++i) {
    int idx = id.wgi * 4 + cw + 128 * i;
    int row = id.sg * 256 - 16 + (i < n ? idx : 16);
    row = row < 0 ? 0 : row;
    const u32* xr = (const u32*)(p.x + (size_t)row * D);
#pragma unroll
    for (int j = 0; j < 5; ++j) u[i][j] = xr[j * 64 + lane];
  }
  float s[3], q[3];
#pragma unroll
  for (int i = 0; i < 3; ++i) {
    s[i] = 0.f;
#pragma unroll
    for (int j = 0; j < 5; ++j) s[i] += bf_lo(u[i][j]) + bf_hi(u[i][j]);
  }
#pragma unroll
  for (int o = 32; o > 0; o >>= 1)
#pragma unroll
    for (int i = 0; i < 3; ++i) s[i] += __shfl_xor(s[i], o);
#pragma unroll
  for (int i = 0; i < 3; ++i) {
    s[i] *= (1.f / D);
    q[i] = 0.f;
#pragma unroll
    for (int j = 0; j < 5; ++j) { float a = bf_lo(u[i][j]) - s[i], b = bf_hi(u[i][j]) - s[i]; q[i] += a * a + b * b; }
  }
#pragma unroll
  for (int o = 32; o > 0; o >>= 1)
#pragma unroll
    for (int i = 0; i < 3; ++i) q[i] += __shfl_xor(q[i], o);
  auto rs = __builtin_amdgcn_make_buffer_rsrc(p.ws, 0, (int)WS_USED, 0x00020000);
  const u32 epoch = __builtin_amdgcn_readfirstlane(ep);
  const u32 tag = (epoch + 1u) * 2654435761u;
  if (lane < n) {
    float m = lane == 0 ? s[0] : lane == 1 ? s[1] : s[2];
    float v = (lane == 0 ? q[0] : lane == 1 ? q[1] : q[2]) * (1.f / D);
    int idx = id.wgi * 4 + cw + 128 * lane;
    u32x4 gq = {__float_as_uint(m), tag, __float_as_uint(v), tag};
    __builtin_amdgcn_raw_buffer_store_b128(gq, rs, WS_SYNC + id.sg * 8192 + SY_STAT + idx * 16, 0, 16);
  }
  return tag;
}
template <bool ISW>
__device__ __forceinline__ void consume_stats(const Params& p, const Ids& id, int lw, int lane, u32 tag) {
  const int t = lw * 64 + lane;
  constexpr int NROW = ISW ? 128 : 144;
  if (lw * 64 < NROW) {
    const int tt = t < NROW ? t : NROW - 1;
    const u32 off = WS_SYNC + id.sg * 8192 + SY_STAT + (id.m2 * 128 + (ISW ? 16 : 0) + tt) * 16;
    auto rs = __builtin_amdgcn_make_buffer_rsrc(p.ws, 0, (int)WS_USED, 0x00020000);
    u32x4 gq;
    for (int spin = 0; spin < SPIN_MAX; ++spin) {
      gq = __builtin_amdgcn_raw_buffer_load_b128(rs, off, 0, 16);
      if (__all(gq[1] == tag && gq[3] == tag)) break;
      __builtin_amdgcn_s_sleep(2);
    }
    float mean = __uint_as_float(gq[0]), var = __uint_as_float(gq[2]);
    float r = rsqrtf(var + 1e-5f);
    if (ISW) r = r * rsqrtf(var * r * r + 1e-5f);
    if (t < NROW) {
      if (ISW) *(LDS_AS f32x2*)(uintptr_t)(L_RS + t * 8) = f32x2{mean * r, r};
      else { *(LDS_AS float*)(uintptr_t)(L_RS + t * 4) = -mean; *(LDS_AS float*)(uintptr_t)(L_RS + 576 + ((t & 1) * 72 + (t >> 1)) * 4) = r; }
    }
  }
  if (ISW && lw == 3 && lane < 48) {
    u32x2 b = *(const u32x2*)(p.b1 + id.g * 192 + lane * 4);
    lds_st_f4(L_BIAS + lane * 16, f32x4{bf_lo(b[0]), bf_hi(b[0]), bf_lo(b[1]), bf_hi(b[1])});
  }
}

template <int V>
__device__ __forceinline__ int gct(int j, int cA, int cB, int cX) {
  using C = CW<V>;
  return j < C::NA ? cA + j : (j < C::NC ? cB + (j - C::NA) : cX + (j - C::NC));
}
template <int V>
__device__ __forceinline__ void store_colsum(f32x4 (&acch)[CW<V>::NH], int lane, int cA, int cB, int cX) {
  if ((lane & 15) == 0) {
#pragma unroll
    for (int j = 0; j < CW<V>::NW; ++j)
      if (hunit<V>(j) >= 0) lds_st_f4(L_CS + (gct<V>(j, cA, cB, cX) * 16 + 4 * (lane >> 4)) * 4, acch[hunit<V>(j) < 0 ? 0 : hunit<V>(j)]);
  }
}
template <int V>
__device__ __forceinline__ void qkv_to_lds(f32x4 (&acc)[4][CW<V>::NC], f32x4 (&acch)[CW<V>::NH], int mh, int lane, int cA, int cB, int cX) {
  using C = CW<V>;
  const int j16 = lane & 15, g4 = lane >> 4;
  float nm[5];
#pragma unroll
  for (int i = 0; i < 5; ++i) nm[i] = *(const LDS_AS float*)(uintptr_t)(L_RS + (((i < 4) ? (1 + 4 * mh + i) : 0) * 16 + j16) * 4);
  const u32 lb = (u32)(j16 * QROW + g4 * 8), lo = lb + (u32)((1 + 4 * mh) * 16 * QROW);
  const u32 csb = L_CS + 16 * g4;
  f32x4 csa[C::NW];
#pragma unroll
  for (int j = 0; j < C::NW; ++j) csa[j] = lds_f4(csb + gct<V>(j, cA, cB, cX) * 64);
  SB();
#pragma unroll
  for (int j = 0; j < C::NW; ++j) {
    const int ct = gct<V>(j, cA, cB, cX);
    f32x4 cs = csa[j];
    f32x2 c01 = {cs[0], cs[1]}, c23 = {cs[2], cs[3]};
u32x2 pk[5];
#pragma unroll
    for (int i = 0; i < 5; ++i) {
      if (i < 4 ? (j < C::NC) : (hunit<V>(j) >= 0)) {
        f32x4 a = (i < 4) ? acc[i < 4 ? i : 0][j < C::NC ? j : 0] : acch[hunit<V>(j) < 0 ? 0 : hunit<V>(j)];
        f32x2 m2 = {nm[i], nm[i]};
        f32x2 v01 = __builtin_elementwise_fma(m2, c01, f32x2{a[0], a[1]});
        f32x2 v23 = __builtin_elementwise_fma(m2, c23, f32x2{a[2], a[3]});
        pk[i] = u32x2{pack_bf16(v01[0], v01[1]), pack_bf16(v23[0], v23[1])};
      }
    }
    SB();
    const u32 lo_c = lo + ct * 32, lb_c = lb + ct * 32;
#pragma unroll
    for (int i = 0; i < 5; ++i) {
      if (i < 4 ? (j < C::NC) : (hunit<V>(j) >= 0)) {
        u32 addr = (i < 4) ? lo_c + (u32)(i * 16 * QROW) : lb_c;
        *(LDS_AS u32x2*)(uintptr_t)addr = pk[i];
      }
    }
  }
}

__device__ __forceinline__ void attention(const Params& p, int mt, int g, int wid  , int lane) {
  const int j16 = lane & 15, hh = lane >> 4;
  const int t = wid * 16 + j16;
  const u32 base = (u32)((16 + t) * QROW + hh * 40);
  const u32 rbase = L_RS + 576 + (u32)(((t & 1) * 72 + ((16 + t) >> 1) - 7) * 4);
  float rk[8];
#pragma unroll
  for (int w = 0; w < 8; ++w) rk[w] = *(const LDS_AS float*)(uintptr_t)(rbase + (7 - w) * 4);
  u32x2 qv[5], kv[8][5], vv[8][5];
#pragma unroll
  for (int i = 0; i < 5; ++i) qv[i] = *(const LDS_AS u32x2*)(uintptr_t)(base + i * 8);
#pragma unroll
  for (int i = 0; i < 5; ++i)
#pragma unroll
    for (int w = 0; w < 8; ++w) kv[w][i] = *(const LDS_AS u32x2*)(uintptr_t)(base + 160 - w * 2 * QROW + i * 8);
  SB();
  float lg[8];
#pragma unroll
  for (int w = 0; w < 8; ++w) lg[w] = 0.f;
#pragma unroll
  for (int i = 0; i < 5; ++i) {
#pragma unroll
    for (int w = 0; w < 8; ++w) {
      vv[w][i] = *(const LDS_AS u32x2*)(uintptr_t)(base + 320 - w * 2 * QROW + i * 8);
      lg[w] = dot2(qv[i][0], kv[w][i][0], lg[w]);
    }
#pragma unroll
    for (int w = 0; w < 8; ++w) lg[w] = dot2(qv[i][1], kv[w][i][1], lg[w]);
    SB();
  }
  const float cq = rk[0] * (0.22360679774997896f * 1.4426950408889634f);
#pragma unroll
  for (int w = 0; w < 8; ++w) lg[w] *= rk[w] * cq;
  if (mt == 0 && wid == 0) {
#pragma unroll
    for (int w = 1; w < 8; ++w) lg[w] = (t - 2 * w >= 0) ? lg[w] : -3.0e38f;
  }
  float mx = fmaxf(fmaxf(fmaxf(lg[0], lg[1]), fmaxf(lg[2], lg[3])), fmaxf(fmaxf(lg[4], lg[5]), fmaxf(lg[6], lg[7])));
  float sum = 0.f;
#pragma unroll
  for (int w = 0; w < 8; ++w) { lg[w] = __builtin_amdgcn_exp2f(lg[w] - mx); sum += lg[w]; }
  float inv = __builtin_amdgcn_rcpf(sum);
  float o[20];
#pragma unroll
  for (int d = 0; d < 20; ++d) o[d] = 0.f;
#pragma unroll
  for (int w = 0; w < 8; ++w) {
    u32 plo = pack_bf16(lg[w] * inv * rk[w], 0.f);
    u32 phi = plo << 16;
#pragma unroll
    for (int i = 0; i < 5; ++i) {
      u32x2 vq = vv[w][i];
      o[4 * i] = dot2(vq[0], plo, o[4 * i]); o[4 * i + 1] = dot2(vq[0], phi, o[4 * i + 1]);
      o[4 * i + 2] = dot2(vq[1], plo, o[4 * i + 2]); o[4 * i + 3] = dot2(vq[1], phi, o[4 * i + 3]);
    }
  }
  u32 d[10];
#pragma unroll
  for (int i = 0; i < 10; ++i) d[i] = pack_bf16(o[2 * i], o[2 * i + 1]);
  const bool odd = hh & 1;
  u32x4 A = odd ? u32x4{d[2], d[3], d[4], d[5]} : u32x4{d[0], d[1], d[2], d[3]};
  u32x4 B = odd ? u32x4{d[6], d[7], d[8], d[9]} : u32x4{d[4], d[5], d[6], d[7]};
  u32x2 Cc = odd ? u32x2{d[0], d[1]} : u32x2{d[8], d[9]};
  int oct0 = 10 * g + 5 * (hh >> 1);
  u32 rb = (u32)((mt * 8 + wid) * RT_STRIDE + j16 * 16);
  u32 offA = rb + (oct0 + (odd ? 3 : 0)) * 256, offB = offA + 256, offC = rb + (oct0 + 2) * 256 + (odd ? 8 : 0);
  auto rs = __builtin_amdgcn_make_buffer_rsrc(p.ws, 0, (int)OH_BYTES, 0x00020000);
  __builtin_amdgcn_raw_buffer_store_b128(A, rs, offA, 0, 16);
  __builtin_amdgcn_raw_buffer_store_b128(B, rs, offB, 0, 16);
  __builtin_amdgcn_raw_buffer_store_b64(Cc, rs, offC, 0, 16);
}

__device__ __forceinline__ float gelu_tanh(float x) {
  const float c1 = -2.f * 0.7978845608028654f * 1.4426950408889634f;
  const float c2 = c1 * 0.044715f;
  float t = x * (c1 + c2 * x * x);
  float e = __builtin_amdgcn_exp2f(t);
  return x * __builtin_amdgcn_rcpf(1.f + e);
}
__device__ __forceinline__ void gelu_store(const Params& p, const Ids& id, f32x4 (&a)[4][6], int mh, int cA, int cB, int lane) {
  const int j16 = lane & 15, g4 = lane >> 4;
  auto rs = __builtin_amdgcn_make_buffer_rsrc(p.ws, 0, (int)OH_BYTES, 0x00020000);
  f32x2 st[4];
#pragma unroll
  for (int i = 0; i < 4; ++i) st[i] = *(const LDS_AS f32x2*)(uintptr_t)(L_RS + ((4 * mh + i) * 16 + j16) * 8);
  const u32 ob = (u32)((id.mt * 8 + 4 * mh) * RT_STRIDE + (80 + id.g * 24) * 256 + lane * 16);
  f32x4 csa[6], ba[6];
#pragma unroll
  for (int j = 0; j < 6; ++j) {
    const int ct = (j < 4) ? cA + j : cB + j - 4;
    csa[j] = lds_f4(L_CS + (ct * 16 + 4 * g4) * 4); ba[j] = lds_f4(L_BIAS + (ct * 16 + 4 * g4) * 4);
  }
  SB();
#pragma unroll
  for (int P = 0; P < 3; ++P) {
    const int ct = (P < 2) ? cA + 2 * P : cB;
    f32x4 cs0 = csa[2 * P], cs1 = csa[2 * P + 1], b0 = ba[2 * P], b1 = ba[2 * P + 1];
#pragma unroll
    for (int i = 0; i < 4; ++i) {
      float v[8];
#pragma unroll
      for (int r = 0; r < 4; ++r) {
        v[r] = gelu_tanh(a[i][2 * P][r] * st[i][1] + (b0[r] - st[i][0] * cs0[r]));
        v[4 + r] = gelu_tanh(a[i][2 * P + 1][r] * st[i][1] + (b1[r] - st[i][0] * cs1[r]));
      }
      u32x4 o = {pack_bf16(v[0], v[1]), pack_bf16(v[2], v[3]), pack_bf16(v[4], v[5]), pack_bf16(v[6], v[7])};
      __builtin_amdgcn_raw_buffer_store_b128(o, rs, ob + (u32)(i * RT_STRIDE + (ct >> 1) * 1024), 0, 16);
    }
  }
}

struct S2L { u32 vbo[4], vbw[4]; i32x4 rsO, rsW; };
__device__ __forceinline__ void s2_setupB(const Params& p, S2L& L, int ctile, int lw, int lane) {
  L.rsO = mk_rsrc(p.wo, D * D * 2); L.rsW = mk_rsrc(p.w2, F * D * 2);
#pragma unroll
  for (int i = 0; i < 4; ++i) {
    int ii = lw + 4 * i;
    int rho = ii * 8 + (lane >> 3), pos = lane & 7;
    int f = ((rho >> 1) & 1) | (((rho >> 3) & 1) << 1);
    int ch = pos ^ (2 * f);
    int e = rho & 7, gg = (rho >> 3) & 3, ks = rho >> 5;
    int kperm = ks * 32 + ((e < 4) ? (4 * gg + e) : (16 + 4 * gg + e - 4));
    L.vbo[i] = (u32)(rho * 1280 + ctile * 128 + ch * 16);
    L.vbw[i] = (u32)(kperm * 1280 + ctile * 128 + ch * 16);
  }
}
__device__ __forceinline__ int s2_rot(const Ids& id, int c) {
  if (c < 12) { c += id.roth; if (c >= 12) c -= 12; }
  else { c += id.roto; if (c >= 17) c -= 5; }
  return c;
}
__device__ __forceinline__ void s2_issueB(const Ids& id, const S2L& L, int c, int slot, int lw) {
  u32 l = S2B_BASE + slot * S2B_SLOT + lw * 1024;
  c = s2_rot(id, c);
  if (c < 12) {
#pragma unroll
    for (int i = 0; i < 4; ++i) dma16(L.rsW, L.vbw[i], c * 128 * 1280, l + i * 4096);
  } else {
#pragma unroll
    for (int i = 0; i < 4; ++i) dma16(L.rsO, L.vbo[i], (c - 12) * 128 * 1280, l + i * 4096);
  }
}
__device__ __forceinline__ void wait_flags(const Params& p, int sg, u32 mtmask, int kind, u32 tag, int lane) {
  const char* f = p.ws + WS_SYNC + sg * 8192 + SY_FLAG + ((lane >> 5) * 64 + kind * 32 + (lane & 31)) * 4;
  const unsigned long long need = ((mtmask & 1) ? 0xFFFFFFFFull : 0ull) | ((mtmask & 2) ? 0xFFFFFFFF00000000ull : 0ull);
  for (int spin = 0; spin < SPIN_MAX; ++spin) {
    u32 v = ld_agent(f);
    unsigned long long ok = __ballot(v == tag);
    if ((ok & need) == need) break;
    __builtin_amdgcn_s_sleep(1);
  }
}
struct FinPre { u32x4 xv[2], bv; };
__device__ __forceinline__ void s2_final_pre(const Params& p, FinPre& f, int nrt, int rt0, int ctile, int tid) {
  const int c8 = tid & 7;
  f.bv = *(const u32x4*)(p.b2 + ctile * 64 + c8 * 8);
#pragma unroll
  for (int it = 0; it < 2; ++it) {
    int r = (tid >> 3) + it * 64;
    r = r < nrt * 16 ? r : (tid >> 3);
    f.xv[it] = *(const u32x4*)(p.x + (size_t)(rt0 * 16 + r) * D + ctile * 64 + c8 * 8);
  }
}
__device__ __forceinline__ constexpr u32 s2_bslot(int p) { return (p % 9) < 5 ? S2B_BASE + (p % 9) * S2B_SLOT : ((p % 9) - 5) * S2B_SLOT; }
__device__ __forceinline__ void s2_loader(const Params& p, const Ids& id, int ctile, u32 mtmask, u32 tag, int lw, int lane) {
  constexpr int NSB = 5, NREG = 12;
  S2L L;
  s2_setupB(p, L, ctile, lw, lane);
#pragma unroll
  for (int c = 0; c < NSB; ++c) s2_issueB(id, L, c, c, lw);
  u32x4 breg[NREG][4];
  {
    auto rsO = __builtin_amdgcn_make_buffer_rsrc((void*)p.wo, 0, D * D * 2, 0x00020000);
    auto rsW = __builtin_amdgcn_make_buffer_rsrc((void*)p.w2, 0, F * D * 2, 0x00020000);
#pragma unroll
    for (int r = 0; r < NREG; ++r) {
      int c = s2_rot(id, NSB + r);
#pragma unroll
      for (int i = 0; i < 4; ++i) {
        wait_vm<THR>();
        if (NSB + r < 12) breg[r][i] = __builtin_amdgcn_raw_buffer_load_b128(rsW, L.vbw[i], c * 128 * 1280, 0);
        else breg[r][i] = __builtin_amdgcn_raw_buffer_load_b128(rsO, L.vbo[i], (c - 12) * 128 * 1280, 0);
      }
    }
  }
  __builtin_amdgcn_s_waitcnt(0x0F70);
  asm volatile("" ::: "memory");
  STAMPW(4, 5);
  const u32 bw = lw * 1024 + lane * 16;
  auto put = [&](int pos) {
#pragma unroll
    for (int i = 0; i < 4; ++i) *(LDS_AS u32x4*)(uintptr_t)(bw + s2_bslot(pos) + i * 4096) = breg[pos - NSB][i];
  };
  bar();
  STAMPW(4, 6);
#pragma unroll
  for (int c = 5; c < 9; ++c) put(c);
  wait_lgkm0();
  bar();
#pragma unroll
  for (int c = 9; c < 14; ++c) put(c);
  wait_lgkm0();
  bar();
#pragma unroll
  for (int c = 14; c < 17; ++c) put(c);
  if (lw == 0) wait_flags(p, id.sg, mtmask, 0, tag, lane);
  wait_lgkm0();
  bar();
}
template <int NRT>
__device__ __forceinline__ void s2_compute_all(const Params& p, const Ids& id, int rt0, int cw, int lane, u32 mtmask, u32 tag, FinPre& fp, int ctile) {
  constexpr int DA = (NRT == 6) ? DA6 : DA4;
  const int j16 = lane & 15, g4 = lane >> 4, q = (lane >> 2) & 3, pp = lane & 3;
  const int f = ((q >> 1) & 1) | ((g4 & 1) << 1);
  const u32 wr = (u32)((cw * 32 + 8 * g4 + q) * 128 + (pp >> 1) * 16 + (pp & 1) * 8);
  u32 wo[4];
#pragma unroll
  for (int j = 0; j < 4; ++j) wo[j] = wr + (j ^ f) * 32;
  auto rsA = __builtin_amdgcn_make_buffer_rsrc(p.ws, 0, (int)OH_BYTES, 0x00020000);
  const u32 va = (u32)(rt0 * RT_STRIDE + cw * 1024 + lane * 16);
  f32x4 acc[NRT][4] = {};
  u32x4 xa[DA + 1][NRT];
  bf16x8 wf[2][4];
  auto loadA = [&](int pos) {
    int c = s2_rot(id, pos);
    u32 so = (c < 12) ? (u32)(20480 + c * 4096) : (u32)((c - 12) * 4096);
#pragma unroll
    for (int i = 0; i < NRT; ++i) xa[pos % (DA + 1)][i] = __builtin_amdgcn_raw_buffer_load_b128(rsA, va + i * RT_STRIDE, so, 16);
  };
  auto loadB = [&](int pos) {
#pragma unroll
    for (int j = 0; j < 4; ++j) wf[pos & 1][j] = lds_tr(wo[j] + s2_bslot(pos), wo[j] + s2_bslot(pos) + 512);
  };
  if (cw == 0) wait_flags(p, id.sg, mtmask, 1, tag, lane);
  bar();
#pragma unroll
  for (int c = 0; c < DA; ++c) loadA(c);
  loadB(0);
#pragma unroll
  for (int c = 0; c < 17; ++c) {
    if (c + DA < 17) loadA(c + DA);
    if (c + 1 < 17) loadB(c + 1);
    SB();
#pragma unroll
    for (int i = 0; i < NRT; ++i)
#pragma unroll
      for (int j = 0; j < 4; ++j) acc[i][j] = mfma(wf[c & 1][j], __builtin_bit_cast(bf16x8, xa[c % (DA + 1)][i]), acc[i][j]);
    SB();
    if (c == 3 || c == 7 || c == 8) { wait_lgkm0(); bar(); }
  }
  STAMPW(0, 7);
  s2_final_pre(p, fp, NRT, rt0, ctile, cw * 64 + lane);
  bar();
  constexpr u32 PR = 272, PH = NRT * 16 * PR;
#pragma unroll
  for (int i = 0; i < NRT; ++i)
#pragma unroll
    for (int j = 0; j < 4; ++j) lds_st_f4(cw * PH + (i * 16 + j16) * PR + (j * 16 + 4 * g4) * 4, acc[i][j]);
  wait_lgkm0();
}
__device__ __forceinline__ void s2_final(const Params& p, const FinPre& f, int nrt, int rt0, int ctile, int tid) {
  const u32 PR = 272, PH = nrt * 16 * PR;
  auto rs = __builtin_amdgcn_make_buffer_rsrc(p.out, 0, S * D * 2, 0x00020000);
  const int c8 = tid & 7;
#pragma unroll
  for (int it = 0; it < 2; ++it) {
    int r = (tid >> 3) + it * 64;
    if (r < nrt * 16) {
      u32 a = (u32)(r * PR + c8 * 32);
      f32x4 p0 = lds_f4(a), p1 = lds_f4(a + 16), q0 = lds_f4(a + PH), q1 = lds_f4(a + PH + 16);
      f32x4 r0 = lds_f4(a + 2 * PH), r1 = lds_f4(a + 2 * PH + 16), s0 = lds_f4(a + 3 * PH), s1 = lds_f4(a + 3 * PH + 16);
      float v[8];
#pragma unroll
      for (int e = 0; e < 4; ++e) { v[e] = (p0[e] + q0[e]) + (r0[e] + s0[e]); v[4 + e] = (p1[e] + q1[e]) + (r1[e] + s1[e]); }
#pragma unroll
      for (int e = 0; e < 4; ++e) { v[2 * e] += bf_lo(f.xv[it][e]) + bf_lo(f.bv[e]); v[2 * e + 1] += bf_hi(f.xv[it][e]) + bf_hi(f.bv[e]); }
      u32x4 o = {pack_bf16(v[0], v[1]), pack_bf16(v[2], v[3]), pack_bf16(v[4], v[5]), pack_bf16(v[6], v[7])};
      __builtin_amdgcn_raw_buffer_store_b128(o, rs, (u32)(((rt0 * 16 + r) * D + ctile * 64 + c8 * 8) * 2), 0, OUT_AUX);
    }
  }
}
__device__ __forceinline__ void publish(const Params& p, const Ids& id, u32 tag, int cw, int lane) {
  wait_vm<0>();
  if (lane == 0) st_agent(p.ws + WS_SYNC + id.sg * 8192 + SY_FLAG + (id.wgi * 4 + cw) * 4, tag);
}
__device__ __forceinline__ void csync() {
  wait_lgkm0();
  LDS_AS u32* cnt = (LDS_AS u32*)(uintptr_t)L_CNT;
  if ((threadIdx.x & 63) == 0) __hip_atomic_fetch_add(cnt, 1u, __ATOMIC_RELAXED, __HIP_MEMORY_SCOPE_WORKGROUP);
  for (int spin = 0; spin < SPIN_MAX; ++spin) {
    u32 c = __builtin_amdgcn_readfirstlane(__hip_atomic_load(cnt, __ATOMIC_RELAXED, __HIP_MEMORY_SCOPE_WORKGROUP));
    if (c >= 4u) break;
  }
  asm volatile("" ::: "memory");
}

__global__ __launch_bounds__(512, 2) void fused_kernel(Params p) {
  extern __shared__ __attribute__((aligned(16))) char smem[];
  const int tid = threadIdx.x, lane = tid & 63, wid = __builtin_amdgcn_readfirstlane(tid >> 6);
  Ids id;
  {
    const int b = blockIdx.x, xcd = b & 7, slot = b >> 3;
    const int xr = xcd >> 1, ml = slot >> 3, r8 = slot & 7;
    id.xc = xcd & 1; id.isw = r8 & 1; id.gi = r8 >> 1; id.g = 4 * id.xc + id.gi;
    id.mt = 4 * xr + ml; id.sg = id.mt >> 1; id.m2 = id.mt & 1;
    id.wgi = id.m2 * 16 + (id.isw ? 8 : 0) + id.g;
    id.rot1 = ((ml * 5 >> 1) + r8 + xr) % 10;
    id.roth = (ml * 3 + r8 + (r8 >> 1) + xr) % 12;
    id.roto = (ml + r8 + xr) % 5;
  }
  STAMPW(0, 0); STAMPW(4, 0);
  int rt0, ctile; u32 mtmask;
  if (id.isw) { ctile = 5 * id.xc + id.gi; rt0 = id.sg * 16 + (id.m2 ? 10 : 0); mtmask = id.m2 ? 2u : 1u; }
  else if (id.m2 == 0) { ctile = 5 * id.xc + id.gi; rt0 = id.sg * 16 + 6; mtmask = 3u; }
  else { ctile = 5 * id.xc + 4; rt0 = id.sg * 16 + 4 * id.gi; mtmask = (id.gi >> 1) ? 2u : 1u; }

  const bool isL = wid >= 4;
  const int w4 = wid & 3, mh = w4 >> 1, nh = w4 & 1;
  const int nrt = id.isw ? 6 : 4;
  FinPre fp;
  if (__builtin_expect(isL, 1)) {
    if (id.isw) s1_loader<true>(p, id, w4, lane); else s1_loader<false>(p, id, w4, lane);
    const u32 epoch = __builtin_amdgcn_readfirstlane(ld_agent(p.ws + WS_SYNC + id.sg * 8192 + SY_EPOCH));
    const u32 tag = (epoch + 1u) * 2654435761u;
    if (id.isw) consume_stats<true>(p, id, w4, lane, tag); else consume_stats<false>(p, id, w4, lane, tag);
    wait_lgkm0();
    bar();
    STAMPW(4, 3);
    s2_loader(p, id, ctile, mtmask, tag, w4, lane);
    if (id.wgi == 0 && w4 == 0 && lane == 0) st_agent(p.ws + WS_SYNC + id.sg * 8192 + SY_EPOCH, epoch + 1u);
    s2_final_pre(p, fp, nrt, rt0, ctile, tid);
    bar();
  } else {
    if (w4 == 0 && lane < 25) {
      u32 val = lane < 24 ? 0x3f803f80u : 0u;
      u32 addr = lane < 24 ? (lane >> 3) * QA_SLOT + (lane & 7) * 16 : L_CNT;
      if (!id.isw || lane == 24) *(LDS_AS u32x4*)(uintptr_t)addr = u32x4{val, val, val, val};
    }
    const u32 tag = produce_stats(p, id, w4, lane);
    wait_lgkm0();
    STAMPW(0, 1);
    if (id.isw) {
      f32x4 acc[4][6] = {};
      f32x4 acch[4] = {};
      const int cA = 6 * nh + 2 * mh, cB = 6 * nh + 4 - 4 * mh;
      s1_compute<2>(p, acc, acch, mh, lane, cA, cB, 0);
      store_colsum<2>(acch, lane, cA, cB, 0);
      wait_lgkm0();
      bar();
      gelu_store(p, id, acc, mh, cA, cB, lane);
    } else {
      if (nh == 0) {
        f32x4 acc[4][8] = {};
        f32x4 acch[2] = {};
        const int cA = 4 * mh, cB = 4 - 4 * mh;
        s1_compute<0>(p, acc, acch, mh, lane, cA, cB, 0);
        store_colsum<0>(acch, lane, cA, cB, 0);
        wait_lgkm0();
        bar();
        STAMPW(0, 12);
        qkv_to_lds<0>(acc, acch, mh, lane, cA, cB, 0);
        STAMPW(0, 13);
      } else {
        f32x4 acc[4][7] = {};
        f32x4 acch[6] = {};
        const int cA = mh ? 11 : 8, cB = mh ? 8 : 12, cX = 2 + 4 * mh;
        s1_compute<1>(p, acc, acch, mh, lane, cA, cB, cX);
        store_colsum<1>(acch, lane, cA, cB, cX);
        wait_lgkm0();
        bar();
        qkv_to_lds<1>(acc, acch, mh, lane, cA, cB, cX);
      }
      csync();
      STAMPW(0, 8);
#pragma unroll 1
      for (int r = 0; r < 2; ++r) { attention(p, id.mt, id.g, 2 * w4 + r, lane); if (r == 0) STAMPW(0, 14); }
    }
    STAMPW(0, 4);
    publish(p, id, tag, w4, lane);
    STAMPW(0, 5);
    wait_lgkm0();
    if (id.isw) s2_compute_all<6>(p, id, rt0, w4, lane, mtmask, tag, fp, ctile);
    else s2_compute_all<4>(p, id, rt0, w4, lane, mtmask, tag, fp, ctile);
  }
  bar();
  s2_final(p, fp, nrt, rt0, ctile, tid);
  STAMPW(0, 9);
}

extern "C" void kernel_launch(void* const* d_in, const int* in_sizes, int n_in, void* d_out, int out_size, void* d_ws, size_t ws_size,
                              hipStream_t stream) {
  Params p;
  p.x = (const u16*)d_in[0]; p.wqkv = (const u16*)d_in[1]; p.wo = (const u16*)d_in[2];
  p.w1 = (const u16*)d_in[4]; p.b1 = (const u16*)d_in[5]; p.w2 = (const u16*)d_in[6]; p.b2 = (const u16*)d_in[7];
  p.out = (u16*)d_out; p.ws = (char*)d_ws;
  (void)hipFuncSetAttribute((const void*)fused_kernel, hipFuncAttributeMaxDynamicSharedMemorySize, L_TOTAL);
  fused_kernel<<<256, 512, L_TOTAL, stream>>>(p);
}
```

```cpp
#include <hip/hip_runtime.h>
#include <stdint.h>

typedef unsigned short u16;
typedef unsigned int u32;
using bf16x8 = __attribute__((ext_vector_type(8))) short;
using s16x4 = __attribute__((ext_vector_type(4))) short;
using f32x4 = __attribute__((ext_vector_type(4))) float;
using u32x4 = __attribute__((ext_vector_type(4))) unsigned;
using u32x2 = __attribute__((ext_vector_type(2))) unsigned;
using i32x4 = __attribute__((ext_vector_type(4))) int;
typedef __bf16 bf16x2_t __attribute__((ext_vector_type(2)));
typedef float f32x2 __attribute__((ext_vector_type(2)));
#define LDS_AS __attribute__((address_space(3)))
#define GLB_AS __attribute__((address_space(1)))

static constexpr int S = 2048, D = 640, F = 1536, NQKV = 1920;
static constexpr int RT_STRIDE = 272 * 256;
static constexpr u32 OH_BYTES = 128u * RT_STRIDE;
static constexpr u32 WS_SYNC = 9u << 20;
static constexpr u32 SY_EPOCH = 0, SY_FLAG = 256, SY_STAT = 1024;
static constexpr u32 WS_USED = WS_SYNC + 8 * 8192;
#define OUT_AUX 16
static constexpr int SPIN_MAX = 1 << 20;

struct Params {
  const u16 *x, *wqkv, *wo, *w1, *b1, *w2, *b2;
  u16* out;
  char* ws;
};

#define SB() __builtin_amdgcn_sched_barrier(0)
#define DA6 2
#define DA4 3
#define THR 6
#define STR2(x) #x
#define STR(x) STR2(x)
#define THROTTLE "s_waitcnt vmcnt(" STR(THR) ")"

__device__ __forceinline__ u32 pack_bf16(float lo, float hi) {
  f32x2 v = {lo, hi};
  bf16x2_t b = __builtin_convertvector(v, bf16x2_t);
  return __builtin_bit_cast(u32, b);
}
__device__ __forceinline__ float bf_lo(u32 u) { return __uint_as_float(u << 16); }
__device__ __forceinline__ float bf_hi(u32 u) { return __uint_as_float(u & 0xffff0000u); }
__device__ __forceinline__ float dot2(u32 a, u32 b, float acc) {
  return __builtin_amdgcn_fdot2_f32_bf16(__builtin_bit_cast(bf16x2_t, a), __builtin_bit_cast(bf16x2_t, b), acc, false);
}
__device__ __forceinline__ i32x4 mk_rsrc(const void* p, u32 bytes) {
  i32x4 r;
  unsigned long long a = (unsigned long long)p;
  r.x = (int)(u32)a; r.y = (int)((u32)(a >> 32) & 0xffffu); r.z = (int)bytes; r.w = 0x00020000;
  return r;
}
__device__ __forceinline__ void dma16(i32x4 rsrc, u32 voff, u32 soff, u32 ldsaddr) {
  asm volatile("s_mov_b32 m0, %3\n\t" THROTTLE "\n\tbuffer_load_dwordx4 %0, %1, %2 offen lds" ::"v"(voff), "s"(rsrc), "s"(soff), "s"(ldsaddr) : "memory");
}
__device__ __forceinline__ void dma16_masked(i32x4 rsrc, u32 voff, u32 soff, u32 ldsaddr, unsigned long long mask) {
  asm volatile("s_mov_b32 m0, %3\n\t" THROTTLE "\n\ts_mov_b64 exec, %4\n\tbuffer_load_dwordx4 %0, %1, %2 offen lds\n\ts_mov_b64 exec, -1" ::"v"(voff), "s"(rsrc), "s"(soff), "s"(ldsaddr), "s"(mask) : "memory");
}
template <int N> __device__ __forceinline__ void wait_vm() { asm volatile("s_waitcnt vmcnt(%0)" ::"i"(N) : "memory"); }
__device__ __forceinline__ void wait_lgkm0() { __builtin_amdgcn_s_waitcnt(0xC07F); asm volatile("" ::: "memory"); }
__device__ __forceinline__ void bar() { asm volatile("s_barrier" ::: "memory"); }
__device__ __forceinline__ bf16x8 lds_b128(u32 addr) { return *(const LDS_AS bf16x8*)(uintptr_t)addr; }
__device__ __forceinline__ f32x4 lds_f4(u32 addr) { return *(const LDS_AS f32x4*)(uintptr_t)addr; }
__device__ __forceinline__ void lds_st_f4(u32 addr, f32x4 v) { *(LDS_AS f32x4*)(uintptr_t)addr = v; }
__device__ __forceinline__ bf16x8 lds_tr(u32 addr0, u32 addr1) {
  s16x4 a = __builtin_amdgcn_ds_read_tr16_b64_v4i16((LDS_AS s16x4*)(uintptr_t)addr0);
  s16x4 b = __builtin_amdgcn_ds_read_tr16_b64_v4i16((LDS_AS s16x4*)(uintptr_t)addr1);
  return __builtin_shufflevector(a, b, 0, 1, 2, 3, 4, 5, 6, 7);
}
__device__ __forceinline__ f32x4 mfma(bf16x8 a, bf16x8 b, f32x4 c) { return __builtin_amdgcn_mfma_f32_16x16x32_bf16(a, b, c, 0, 0, 0); }
__device__ __forceinline__ u32 ld_agent(const char* p) { return __hip_atomic_load((const GLB_AS u32*)p, __ATOMIC_RELAXED, __HIP_MEMORY_SCOPE_AGENT); }
__device__ __forceinline__ void st_agent(char* p, u32 v) { __hip_atomic_store((GLB_AS u32*)p, v, __ATOMIC_RELAXED, __HIP_MEMORY_SCOPE_AGENT); }

static constexpr u32 QA_SLOT = 18432, QB_SLOT = 15360, QB_BASE = 3 * QA_SLOT;
static constexpr u32 WA_SLOT = 16384, WB_SLOT = 13312, WB_BASE = 3 * WA_SLOT;
static constexpr u32 S2B_BASE = 73728, S2B_SLOT = 16384;
static constexpr u32 L_CS = 155648;
static constexpr u32 L_RS = L_CS + 1024;
static constexpr u32 L_BIAS = L_RS + 1152;
static constexpr u32 L_JUNK = L_BIAS + 768;
static constexpr u32 L_CNT = L_JUNK + 1024;
static constexpr u32 L_TOTAL = L_CNT + 16;
static constexpr u32 QROW = 496;

struct Ids { int mt, g, sg, m2, wgi, xc, gi, rot1, roth, roto; bool isw; };

template <int V> struct CW;
template <> struct CW<0> { static constexpr int NC = 8, NW = 8, NH = 2, NA = 4, BROW = 480, NX = 5; static constexpr u32 ASLOT = QA_SLOT, BSLOT = QB_SLOT, BBASE = QB_BASE, NSB = 6; };
template <> struct CW<1> { static constexpr int NC = 7, NW = 9, NH = 6, NA = 4, BROW = 480, NX = 5; static constexpr u32 ASLOT = QA_SLOT, BSLOT = QB_SLOT, BBASE = QB_BASE, NSB = 6; };
template <> struct CW<2> { static constexpr int NC = 6, NW = 6, NH = 4, NA = 4, BROW = 416, NX = 4; static constexpr u32 ASLOT = WA_SLOT, BSLOT = WB_SLOT, BBASE = WB_BASE, NSB = 7; };
template <int V> __device__ __forceinline__ constexpr int hunit(int j) {
  return V == 0 ? (j < 2 ? j : -1) : V == 1 ? (j < 4 ? j : (j >= 7 ? j - 3 : -1)) : (j < 4 ? j : -1);
}
template <int V> __device__ __forceinline__ u32 waddr(int j, u32 wA, u32 wB, u32 wX) {
  using C = CW<V>;
  return j < C::NA ? wA + j * 32 : (j < C::NC ? wB + (j - C::NA) * 32 : wX + (j - C::NC) * 32);
}

template <int V>
__device__ __forceinline__ void kstep(f32x4 (&acc)[4][CW<V>::NC], f32x4 (&acch)[CW<V>::NH], bf16x8 (&xc)[CW<V>::NX], bf16x8 (&xn)[CW<V>::NX],
                                      bf16x8 (&w)[CW<V>::NW], u32 xo, u32 xh, u32 wA, u32 wB, u32 wX, u32 wlast) {
  using C = CW<V>;
  const short one = (short)0x3f80;
  const bf16x8 ones = {one, one, one, one, one, one, one, one};
#pragma unroll
  for (int j = 0; j < C::NW; ++j) {
    if (j < C::NC) {
#pragma unroll
      for (int i = 0; i < 4; ++i) {
        acc[i][j] = mfma(w[j], xc[i], acc[i][j]);
        if (i == 0 && j < C::NX) xn[j] = lds_b128(j < 4 ? xo + j * 2048 : xh);
        if (i == 2) {
          u32 a = (j == 0) ? wlast : waddr<V>(j - 1, wA, wB, wX);
          w[j == 0 ? C::NW - 1 : j - 1] = lds_tr(a, a + 16 * C::BROW);
        }
        SB();
      }
    }
    if (hunit<V>(j) >= 0) acch[hunit<V>(j) < 0 ? 0 : hunit<V>(j)] = mfma(w[j], V == 2 ? ones : xc[C::NX - 1], acch[hunit<V>(j) < 0 ? 0 : hunit<V>(j)]);
    if (j >= C::NC) { u32 a = waddr<V>(j - 1, wA, wB, wX); w[j - 1] = lds_tr(a, a + 16 * C::BROW); }
    SB();
  }
}

template <int V>
__device__ __forceinline__ void s1_compute(const Params& p, f32x4 (&acc)[4][CW<V>::NC], f32x4 (&acch)[CW<V>::NH], int mh, int lane, int cA, int cB, int cX) {
  using C = CW<V>;
  const int j16 = lane & 15, g4 = lane >> 4, q = (lane >> 2) & 3, pp = lane & 3;
  const int rt0 = (V == 2) ? 4 * mh : 1 + 4 * mh;
  const u32 xl = (u32)(j16 * 128 + ((g4 ^ ((j16 >> 1) & 7)) * 16));
  u32 xo0 = xl + rt0 * 2048, xo1 = (xl ^ 64u) + rt0 * 2048, xh0 = xl, xh1 = xl ^ 64u;
  const u32 wl = (u32)(C::BBASE + (4 * g4 + q) * C::BROW + pp * 8);
  u32 wA = wl + cA * 32, wB = wl + cB * 32, wX = wl + cX * 32;
  bf16x8 xa[C::NX], xb[C::NX], w[C::NW];
  bar();
#pragma unroll
  for (int i = 0; i < C::NX; ++i) xa[i] = lds_b128(i < 4 ? xo0 + i * 2048 : xh0);
#pragma unroll
  for (int j = 0; j < C::NW; ++j) { u32 a = waddr<V>(j, wA, wB, wX); w[j] = lds_tr(a, a + 16 * C::BROW); }
  wait_lgkm0();
  int bs = 0, as = 0;
  u32 wlast;
  auto stepB = [&]() {
    u32 dlt = (bs == (int)C::NSB - 1) ? (u32)(-(int)((C::NSB - 1) * C::BSLOT)) : C::BSLOT;
    bs = (bs == (int)C::NSB - 1) ? 0 : bs + 1;
    wlast = waddr<V>(C::NW - 1, wA, wB, wX);
    wA += dlt; wB += dlt; if (V == 1) wX += dlt;
  };
#pragma unroll 1
  for (int it = 0; it < 10; ++it) {
    stepB();
    bar();
    kstep<V>(acc, acch, xa, xb, w, xo1, xh1, wA, wB, wX, wlast);
    u32 dA = (as == 2) ? (u32)(-(int)(2 * C::ASLOT)) : C::ASLOT;
    as = (as == 2) ? 0 : as + 1;
    xo0 += dA; xo1 += dA; if (V != 2) { xh0 += dA; xh1 += dA; }
    stepB();
    bar();
    kstep<V>(acc, acch, xb, xa, w, xo0, xh0, wA, wB, wX, wlast);
  }
}

template <bool ISW>
__device__ __forceinline__ void s1_loader(const Params& p, const Ids& id, int lw, int lane) {
  constexpr int NA = ISW ? 4 : 5, NAR = ISW ? 16 : 18, NBR = ISW ? 13 : 15, NSB = ISW ? 7 : 6, WN = ISW ? 16 : 17;
  constexpr u32 ASLOT = ISW ? WA_SLOT : QA_SLOT, BSLOT = ISW ? WB_SLOT : QB_SLOT, BBASE = ISW ? WB_BASE : QB_BASE;
  constexpr u32 WROW = ISW ? 3072 : 3840;
  const i32x4 rsA = mk_rsrc(p.x, S * D * 2);
  const i32x4 rsB = ISW ? mk_rsrc(p.w1, D * F * 2) : mk_rsrc(p.wqkv, D * NQKV * 2);
  const int row0 = id.mt * 128 - (ISW ? 0 : 16);
  u32 va[NA], vb[4];
#pragma unroll
  for (int i = 0; i < NA; ++i) {
    int ii = lw + 4 * i;
    int r = ii * 8 + (lane >> 3), pos = lane & 7;
    int ch = pos ^ ((r >> 1) & 7);
    va[i] = (ii < NAR) ? (u32)((row0 + r) * 1280 + ch * 16) : (u32)(id.mt * 128 * 1280);
  }
  const unsigned long long m0mask = (!ISW && lw == 0) ? ~0xFFull : ~0ull;
  auto issueA = [&](int a, int slot) {
    u32 l = slot * ASLOT + lw * 1024;
    a += id.rot1; if (a >= 10) a -= 10;
    dma16_masked(rsA, va[0], a * 128, l, m0mask);
#pragma unroll
    for (int i = 1; i < NA; ++i) dma16(rsA, va[i], a * 128, (lw + 4 * i < NAR) ? l + i * 4096 : L_JUNK);
  };
  issueA(0, 0);
  SB();
#pragma unroll
  for (int i = 0; i < 4; ++i) {
    int ii = lw + 4 * i;
    int idx = ii * 64 + lane;
    constexpr int CPR = ISW ? 26 : 30;
    int rho = idx / CPR, pos = idx - rho * CPR;
    int kap = 8 * ((rho >> 2) & 3) + (rho & 3) + 4 * ((rho >> 4) & 1);
    u32 v;
    if (ISW) { int pc = pos < 24 ? pos : 23; v = (u32)(kap * 3072 + id.g * 384 + pc * 16); }
    else { int seg = pos / 10, off = pos - seg * 10; v = (u32)(kap * 3840 + seg * 1280 + id.g * 160 + off * 16); }
    vb[i] = (ii < NBR) ? v : 0u;
  }
  auto issueB = [&](int t, int slot) {
    u32 l = BBASE + slot * BSLOT + lw * 1024;
    t += 2 * id.rot1; if (t >= 20) t -= 20;
#pragma unroll
    for (int i = 0; i < 4; ++i) dma16(rsB, vb[i], t * 32 * WROW, (lw + 4 * i < NBR) ? l + i * 4096 : L_JUNK);
  };
  issueB(0, 0); issueB(1, 1);
  wait_vm<4>();
  bar();
  issueA(1, 1); issueB(2, 2);
  wait_vm<NA + 4>();
  bar();
  int bs = 0, as = 0;
#pragma unroll 1
  for (int t = 0; t < 19; ++t) {
    if (t == 0) { issueB(3, 3); issueA(2, 2); }
    if (t == 1) { issueB(4, 4); issueB(5, 5); }
    if (ISW && t == 2) issueB(6, 6);
    if (t >= 2 && !(t & 1) && t <= 14) { issueA(t / 2 + 2, as); as = (as == 2) ? 0 : as + 1; }
    if (t >= 1 && t + NSB - 1 <= 19) { issueB(t + NSB - 1, bs); bs = (bs == NSB - 1) ? 0 : bs + 1; }
    if (t == 0) wait_vm<NA + 4>(); else if (t < 13) wait_vm<WN>(); else wait_vm<0>();
    bar();
  }
}

__device__ __forceinline__ u32 produce_stats(const Params& p, const Ids& id, int cw, int lane) {
  const int n = (id.wgi < 4) ? 3 : 2;
  const u32 ep = ld_agent(p.ws + WS_SYNC + id.sg * 8192 + SY_EPOCH);
  u32 u[3][5];
#pragma unroll
  for (int i = 0; i < 3; ++i) {
    int idx = id.wgi * 4 + cw + 128 * i;
    int row = id.sg * 256 - 16 + (i < n ? idx : 16);
    row = row < 0 ? 0 : row;
    const u32* xr = (const u32*)(p.x + (size_t)row * D);
#pragma unroll
    for (int j = 0; j < 5; ++j) u[i][j] = xr[j * 64 + lane];
  }
  float s[3], q[3];
#pragma unroll
  for (int i = 0; i < 3; ++i) {
    s[i] = 0.f;
#pragma unroll
    for (int j = 0; j < 5; ++j) s[i] += bf_lo(u[i][j]) + bf_hi(u[i][j]);
  }
#pragma unroll
  for (int o = 32; o > 0; o >>= 1)
#pragma unroll
    for (int i = 0; i < 3; ++i) s[i] += __shfl_xor(s[i], o);
#pragma unroll
  for (int i = 0; i < 3; ++i) {
    s[i] *= (1.f / D);
    q[i] = 0.f;
#pragma unroll
    for (int j = 0; j < 5; ++j) { float a = bf_lo(u[i][j]) - s[i], b = bf_hi(u[i][j]) - s[i]; q[i] += a * a + b * b; }
  }
#pragma unroll
  for (int o = 32; o > 0; o >>= 1)
#pragma unroll
    for (int i = 0; i < 3; ++i) q[i] += __shfl_xor(q[i], o);
  auto rs = __builtin_amdgcn_make_buffer_rsrc(p.ws, 0, (int)WS_USED, 0x00020000);
  const u32 epoch = __builtin_amdgcn_readfirstlane(ep);
  const u32 tag = (epoch + 1u) * 2654435761u;
  if (lane < n) {
    float m = lane == 0 ? s[0] : lane == 1 ? s[1] : s[2];
    float v = (lane == 0 ? q[0] : lane == 1 ? q[1] : q[2]) * (1.f / D);
    int idx = id.wgi * 4 + cw + 128 * lane;
    u32x4 gq = {__float_as_uint(m), tag, __float_as_uint(v), tag};
    __builtin_amdgcn_raw_buffer_store_b128(gq, rs, WS_SYNC + id.sg * 8192 + SY_STAT + idx * 16, 0, 16);
  }
  return tag;
}
template <bool ISW>
__device__ __forceinline__ void consume_stats(const Params& p, const Ids& id, int lw, int lane, u32 tag) {
  const int t = lw * 64 + lane;
  constexpr int NROW = ISW ? 128 : 144;
  if (lw * 64 < NROW) {
    const int tt = t < NROW ? t : NROW - 1;
    const u32 off = WS_SYNC + id.sg * 8192 + SY_STAT + (id.m2 * 128 + (ISW ? 16 : 0) + tt) * 16;
    auto rs = __builtin_amdgcn_make_buffer_rsrc(p.ws, 0, (int)WS_USED, 0x00020000);
    u32x4 gq;
    for (int spin = 0; spin < SPIN_MAX; ++spin) {
      gq = __builtin_amdgcn_raw_buffer_load_b128(rs, off, 0, 16);
      if (__all(gq[1] == tag && gq[3] == tag)) break;
      __builtin_amdgcn_s_sleep(2);
    }
    float mean = __uint_as_float(gq[0]), var = __uint_as_float(gq[2]);
    float r = rsqrtf(var + 1e-5f);
    if (ISW) r = r * rsqrtf(var * r * r + 1e-5f);
    if (t < NROW) {
      if (ISW) *(LDS_AS f32x2*)(uintptr_t)(L_RS + t * 8) = f32x2{mean * r, r};
      else { *(LDS_AS float*)(uintptr_t)(L_RS + t * 4) = -mean; *(LDS_AS float*)(uintptr_t)(L_RS + 576 + ((t & 1) * 72 + (t >> 1)) * 4) = r; }
    }
  }
  if (ISW && lw == 3 && lane < 48) {
    u32x2 b = *(const u32x2*)(p.b1 + id.g * 192 + lane * 4);
    lds_st_f4(L_BIAS + lane * 16, f32x4{bf_lo(b[0]), bf_hi(b[0]), bf_lo(b[1]), bf_hi(b[1])});
  }
}

template <int V>
__device__ __forceinline__ int gct(int j, int cA, int cB, int cX) {
  using C = CW<V>;
  return j < C::NA ? cA + j : (j < C::NC ? cB + (j - C::NA) : cX + (j - C::NC));
}
template <int V>
__device__ __forceinline__ void store_colsum(f32x4 (&acch)[CW<V>::NH], int lane, int cA, int cB, int cX) {
  if ((lane & 15) == 0) {
#pragma unroll
    for (int j = 0; j < CW<V>::NW; ++j)
      if (hunit<V>(j) >= 0) lds_st_f4(L_CS + (gct<V>(j, cA, cB, cX) * 16 + 4 * (lane >> 4)) * 4, acch[hunit<V>(j) < 0 ? 0 : hunit<V>(j)]);
  }
}
template <int V>
__device__ __forceinline__ void qkv_to_lds(f32x4 (&acc)[4][CW<V>::NC], f32x4 (&acch)[CW<V>::NH], int mh, int lane, int cA, int cB, int cX) {
  using C = CW<V>;
  const int j16 = lane & 15, g4 = lane >> 4;
  float nm[5];
#pragma unroll
  for (int i = 0; i < 5; ++i) nm[i] = *(const LDS_AS float*)(uintptr_t)(L_RS + (((i < 4) ? (1 + 4 * mh + i) : 0) * 16 + j16) * 4);
  const u32 lb = (u32)(j16 * QROW + g4 * 8), lo = lb + (u32)((1 + 4 * mh) * 16 * QROW);
  const u32 csb = L_CS + 16 * g4;
  f32x4 csa[C::NW];
#pragma unroll
  for (int j = 0; j < C::NW; ++j) csa[j] = lds_f4(csb + gct<V>(j, cA, cB, cX) * 64);
  SB();
#pragma unroll
  for (int j = 0; j < C::NW; ++j) {
    const int ct = gct<V>(j, cA, cB, cX);
    f32x4 cs = csa[j];
    f32x2 c01 = {cs[0], cs[1]}, c23 = {cs[2], cs[3]};
u32x2 pk[5];
#pragma unroll
    for (int i = 0; i < 5; ++i) {
      if (i < 4 ? (j < C::NC) : (hunit<V>(j) >= 0)) {
        f32x4 a = (i < 4) ? acc[i < 4 ? i : 0][j < C::NC ? j : 0] : acch[hunit<V>(j) < 0 ? 0 : hunit<V>(j)];
        f32x2 m2 = {nm[i], nm[i]};
        f32x2 v01 = __builtin_elementwise_fma(m2, c01, f32x2{a[0], a[1]});
        f32x2 v23 = __builtin_elementwise_fma(m2, c23, f32x2{a[2], a[3]});
        pk[i] = u32x2{pack_bf16(v01[0], v01[1]), pack_bf16(v23[0], v23[1])};
      }
    }
    SB();
    const u32 lo_c = lo + ct * 32, lb_c = lb + ct * 32;
#pragma unroll
    for (int i = 0; i < 5; ++i) {
      if (i < 4 ? (j < C::NC) : (hunit<V>(j) >= 0)) {
        u32 addr = (i < 4) ? lo_c + (u32)(i * 16 * QROW) : lb_c;
        *(LDS_AS u32x2*)(uintptr_t)addr = pk[i];
      }
    }
  }
}

__device__ __forceinline__ void attention(const Params& p, int mt, int g, int wid  , int lane) {
  const int j16 = lane & 15, hh = lane >> 4;
  const int t = wid * 16 + j16;
  const u32 base = (u32)((16 + t) * QROW + hh * 40);
  const u32 rbase = L_RS + 576 + (u32)(((t & 1) * 72 + ((16 + t) >> 1) - 7) * 4);
  float rk[8];
#pragma unroll
  for (int w = 0; w < 8; ++w) rk[w] = *(const LDS_AS float*)(uintptr_t)(rbase + (7 - w) * 4);
  u32x2 qv[5], kv[8][5], vv[8][5];
#pragma unroll
  for (int i = 0; i < 5; ++i) qv[i] = *(const LDS_AS u32x2*)(uintptr_t)(base + i * 8);
#pragma unroll
  for (int i = 0; i < 5; ++i)
#pragma unroll
    for (int w = 0; w < 8; ++w) kv[w][i] = *(const LDS_AS u32x2*)(uintptr_t)(base + 160 - w * 2 * QROW + i * 8);
  SB();
  float lg[8];
#pragma unroll
  for (int w = 0; w < 8; ++w) lg[w] = 0.f;
#pragma unroll
  for (int i = 0; i < 5; ++i) {
#pragma unroll
    for (int w = 0; w < 8; ++w) {
      vv[w][i] = *(const LDS_AS u32x2*)(uintptr_t)(base + 320 - w * 2 * QROW + i * 8);
      lg[w] = dot2(qv[i][0], kv[w][i][0], lg[w]);
    }
#pragma unroll
    for (int w = 0; w < 8; ++w) lg[w] = dot2(qv[i][1], kv[w][i][1], lg[w]);
    SB();
  }
  const float cq = rk[0] * (0.22360679774997896f * 1.4426950408889634f);
#pragma unroll
  for (int w = 0; w < 8; ++w) lg[w] *= rk[w] * cq;
  if (mt == 0 && wid == 0) {
#pragma unroll
    for (int w = 1; w < 8; ++w) lg[w] = (t - 2 * w >= 0) ? lg[w] : -3.0e38f;
  }
  float mx = fmaxf(fmaxf(fmaxf(lg[0], lg[1]), fmaxf(lg[2], lg[3])), fmaxf(fmaxf(lg[4], lg[5]), fmaxf(lg[6], lg[7])));
  float sum = 0.f;
#pragma unroll
  for (int w = 0; w < 8; ++w) { lg[w] = __builtin_amdgcn_exp2f(lg[w] - mx); sum += lg[w]; }
  float inv = __builtin_amdgcn_rcpf(sum);
  float o[20];
#pragma unroll
  for (int d = 0; d < 20; ++d) o[d] = 0.f;
#pragma unroll
  for (int w = 0; w < 8; ++w) {
    u32 plo = pack_bf16(lg[w] * inv * rk[w], 0.f);
    u32 phi = plo << 16;
#pragma unroll
    for (int i = 0; i < 5; ++i) {
      u32x2 vq = vv[w][i];
      o[4 * i] = dot2(vq[0], plo, o[4 * i]); o[4 * i + 1] = dot2(vq[0], phi, o[4 * i + 1]);
      o[4 * i + 2] = dot2(vq[1], plo, o[4 * i + 2]); o[4 * i + 3] = dot2(vq[1], phi, o[4 * i + 3]);
    }
  }
  u32 d[10];
#pragma unroll
  for (int i = 0; i < 10; ++i) d[i] = pack_bf16(o[2 * i], o[2 * i + 1]);
  const bool odd = hh & 1;
  u32x4 A = odd ? u32x4{d[2], d[3], d[4], d[5]} : u32x4{d[0], d[1], d[2], d[3]};
  u32x4 B = odd ? u32x4{d[6], d[7], d[8], d[9]} : u32x4{d[4], d[5], d[6], d[7]};
  u32x2 Cc = odd ? u32x2{d[0], d[1]} : u32x2{d[8], d[9]};
  int oct0 = 10 * g + 5 * (hh >> 1);
  u32 rb = (u32)((mt * 8 + wid) * RT_STRIDE + j16 * 16);
  u32 offA = rb + (oct0 + (odd ? 3 : 0)) * 256, offB = offA + 256, offC = rb + (oct0 + 2) * 256 + (odd ? 8 : 0);
  auto rs = __builtin_amdgcn_make_buffer_rsrc(p.ws, 0, (int)OH_BYTES, 0x00020000);
  __builtin_amdgcn_raw_buffer_store_b128(A, rs, offA, 0, 16);
  __builtin_amdgcn_raw_buffer_store_b128(B, rs, offB, 0, 16);
  __builtin_amdgcn_raw_buffer_store_b64(Cc, rs, offC, 0, 16);
}

__device__ __forceinline__ float gelu_tanh(float x) {
  const float c1 = -2.f * 0.7978845608028654f * 1.4426950408889634f;
  const float c2 = c1 * 0.044715f;
  float t = x * (c1 + c2 * x * x);
  float e = __builtin_amdgcn_exp2f(t);
  return x * __builtin_amdgcn_rcpf(1.f + e);
}
__device__ __forceinline__ void gelu_store(const Params& p, const Ids& id, f32x4 (&a)[4][6], int mh, int cA, int cB, int lane) {
  const int j16 = lane & 15, g4 = lane >> 4;
  auto rs = __builtin_amdgcn_make_buffer_rsrc(p.ws, 0, (int)OH_BYTES, 0x00020000);
  f32x2 st[4];
#pragma unroll
  for (int i = 0; i < 4; ++i) st[i] = *(const LDS_AS f32x2*)(uintptr_t)(L_RS + ((4 * mh + i) * 16 + j16) * 8);
  const u32 ob = (u32)((id.mt * 8 + 4 * mh) * RT_STRIDE + (80 + id.g * 24) * 256 + lane * 16);
  f32x4 csa[6], ba[6];
#pragma unroll
  for (int j = 0; j < 6; ++j) {
    const int ct = (j < 4) ? cA + j : cB + j - 4;
    csa[j] = lds_f4(L_CS + (ct * 16 + 4 * g4) * 4); ba[j] = lds_f4(L_BIAS + (ct * 16 + 4 * g4) * 4);
  }
  SB();
#pragma unroll
  for (int P = 0; P < 3; ++P) {
    const int ct = (P < 2) ? cA + 2 * P : cB;
    f32x4 cs0 = csa[2 * P], cs1 = csa[2 * P + 1], b0 = ba[2 * P], b1 = ba[2 * P + 1];
#pragma unroll
    for (int i = 0; i < 4; ++i) {
      float v[8];
#pragma unroll
      for (int r = 0; r < 4; ++r) {
        v[r] = gelu_tanh(a[i][2 * P][r] * st[i][1] + (b0[r] - st[i][0] * cs0[r]));
        v[4 + r] = gelu_tanh(a[i][2 * P + 1][r] * st[i][1] + (b1[r] - st[i][0] * cs1[r]));
      }
      u32x4 o = {pack_bf16(v[0], v[1]), pack_bf16(v[2], v[3]), pack_bf16(v[4], v[5]), pack_bf16(v[6], v[7])};
      __builtin_amdgcn_raw_buffer_store_b128(o, rs, ob + (u32)(i * RT_STRIDE + (ct >> 1) * 1024), 0, 16);
    }
  }
}

struct S2L { u32 vbo[4], vbw[4]; i32x4 rsO, rsW; };
__device__ __forceinline__ void s2_setupB(const Params& p, S2L& L, int ctile, int lw, int lane) {
  L.rsO = mk_rsrc(p.wo, D * D * 2); L.rsW = mk_rsrc(p.w2, F * D * 2);
#pragma unroll
  for (int i = 0; i < 4; ++i) {
    int ii = lw + 4 * i;
    int rho = ii * 8 + (lane >> 3), pos = lane & 7;
    int f = ((rho >> 1) & 1) | (((rho >> 3) & 1) << 1);
    int ch = pos ^ (2 * f);
    int e = rho & 7, gg = (rho >> 3) & 3, ks = rho >> 5;
    int kperm = ks * 32 + ((e < 4) ? (4 * gg + e) : (16 + 4 * gg + e - 4));
    L.vbo[i] = (u32)(rho * 1280 + ctile * 128 + ch * 16);
    L.vbw[i] = (u32)(kperm * 1280 + ctile * 128 + ch * 16);
  }
}
__device__ __forceinline__ int s2_rot(const Ids& id, int c) {
  if (c < 12) { c += id.roth; if (c >= 12) c -= 12; }
  else { c += id.roto; if (c >= 17) c -= 5; }
  return c;
}
__device__ __forceinline__ void s2_issueB(const Ids& id, const S2L& L, int c, int slot, int lw) {
  u32 l = S2B_BASE + slot * S2B_SLOT + lw * 1024;
  c = s2_rot(id, c);
  if (c < 12) {
#pragma unroll
    for (int i = 0; i < 4; ++i) dma16(L.rsW, L.vbw[i], c * 128 * 1280, l + i * 4096);
  } else {
#pragma unroll
    for (int i = 0; i < 4; ++i) dma16(L.rsO, L.vbo[i], (c - 12) * 128 * 1280, l + i * 4096);
  }
}
__device__ __forceinline__ void wait_flags(const Params& p, int sg, u32 mtmask, int kind, u32 tag, int lane) {
  const char* f = p.ws + WS_SYNC + sg * 8192 + SY_FLAG + ((lane >> 5) * 64 + kind * 32 + (lane & 31)) * 4;
  const unsigned long long need = ((mtmask & 1) ? 0xFFFFFFFFull : 0ull) | ((mtmask & 2) ? 0xFFFFFFFF00000000ull : 0ull);
  for (int spin = 0; spin < SPIN_MAX; ++spin) {
    u32 v = ld_agent(f);
    unsigned long long ok = __ballot(v == tag);
    if ((ok & need) == need) break;
    __builtin_amdgcn_s_sleep(10);
  }
}
struct FinPre { u32x4 xv[2], bv; };
__device__ __forceinline__ void s2_final_pre(const Params& p, FinPre& f, int nrt, int rt0, int ctile, int tid) {
  const int c8 = tid & 7;
  f.bv = *(const u32x4*)(p.b2 + ctile * 64 + c8 * 8);
#pragma unroll
  for (int it = 0; it < 2; ++it) {
    int r = (tid >> 3) + it * 64;
    r = r < nrt * 16 ? r : (tid >> 3);
    f.xv[it] = *(const u32x4*)(p.x + (size_t)(rt0 * 16 + r) * D + ctile * 64 + c8 * 8);
  }
}
__device__ __forceinline__ constexpr u32 s2_bslot(int p) { return (p % 9) < 5 ? S2B_BASE + (p % 9) * S2B_SLOT : ((p % 9) - 5) * S2B_SLOT; }
__device__ __forceinline__ void s2_loader(const Params& p, const Ids& id, int ctile, u32 mtmask, u32 tag, int lw, int lane) {
  constexpr int NSB = 5, NREG = 12;
  S2L L;
  s2_setupB(p, L, ctile, lw, lane);
#pragma unroll
  for (int c = 0; c < NSB; ++c) s2_issueB(id, L, c, c, lw);
  u32x4 breg[NREG][4];
  {
    auto rsO = __builtin_amdgcn_make_buffer_rsrc((void*)p.wo, 0, D * D * 2, 0x00020000);
    auto rsW = __builtin_amdgcn_make_buffer_rsrc((void*)p.w2, 0, F * D * 2, 0x00020000);
#pragma unroll
    for (int r = 0; r < NREG; ++r) {
      int c = s2_rot(id, NSB + r);
#pragma unroll
      for (int i = 0; i < 4; ++i) {
        wait_vm<THR>();
        if (NSB + r < 12) breg[r][i] = __builtin_amdgcn_raw_buffer_load_b128(rsW, L.vbw[i], c * 128 * 1280, 0);
        else breg[r][i] = __builtin_amdgcn_raw_buffer_load_b128(rsO, L.vbo[i], (c - 12) * 128 * 1280, 0);
      }
    }
  }
  __builtin_amdgcn_s_waitcnt(0x0F70);
  asm volatile("" ::: "memory");
  const u32 bw = lw * 1024 + lane * 16;
  auto put = [&](int pos) {
#pragma unroll
    for (int i = 0; i < 4; ++i) *(LDS_AS u32x4*)(uintptr_t)(bw + s2_bslot(pos) + i * 4096) = breg[pos - NSB][i];
  };
  bar();
#pragma unroll
  for (int c = 5; c < 9; ++c) put(c);
  wait_lgkm0();
  bar();
#pragma unroll
  for (int c = 9; c < 14; ++c) put(c);
  wait_lgkm0();
  bar();
#pragma unroll
  for (int c = 14; c < 17; ++c) put(c);
  if (lw == 0) wait_flags(p, id.sg, mtmask, 0, tag, lane);
  wait_lgkm0();
  bar();
}
template <int NRT>
__device__ __forceinline__ void s2_compute_all(const Params& p, const Ids& id, int rt0, int cw, int lane, u32 mtmask, u32 tag, FinPre& fp, int ctile) {
  constexpr int DA = (NRT == 6) ? DA6 : DA4;
  const int j16 = lane & 15, g4 = lane >> 4, q = (lane >> 2) & 3, pp = lane & 3;
  const int f = ((q >> 1) & 1) | ((g4 & 1) << 1);
  const u32 wr = (u32)((cw * 32 + 8 * g4 + q) * 128 + (pp >> 1) * 16 + (pp & 1) * 8);
  u32 wo[4];
#pragma unroll
  for (int j = 0; j < 4; ++j) wo[j] = wr + (j ^ f) * 32;
  auto rsA = __builtin_amdgcn_make_buffer_rsrc(p.ws, 0, (int)OH_BYTES, 0x00020000);
  const u32 va = (u32)(rt0 * RT_STRIDE + cw * 1024 + lane * 16);
  f32x4 acc[NRT][4] = {};
  u32x4 xa[DA + 1][NRT];
  bf16x8 wf[2][4];
  auto loadA = [&](int pos) {
    int c = s2_rot(id, pos);
    u32 so = (c < 12) ? (u32)(20480 + c * 4096) : (u32)((c - 12) * 4096);
#pragma unroll
    for (int i = 0; i < NRT; ++i) xa[pos % (DA + 1)][i] = __builtin_amdgcn_raw_buffer_load_b128(rsA, va + i * RT_STRIDE, so, 16);
  };
  auto loadB = [&](int pos) {
#pragma unroll
    for (int j = 0; j < 4; ++j) wf[pos & 1][j] = lds_tr(wo[j] + s2_bslot(pos), wo[j] + s2_bslot(pos) + 512);
  };
  if (cw == 0) wait_flags(p, id.sg, mtmask, 1, tag, lane);
  bar();
#pragma unroll
  for (int c = 0; c < DA; ++c) loadA(c);
  loadB(0);
#pragma unroll
  for (int c = 0; c < 17; ++c) {
    if (c + DA < 17) loadA(c + DA);
    if (c + 1 < 17) loadB(c + 1);
    SB();
#pragma unroll
    for (int i = 0; i < NRT; ++i)
#pragma unroll
      for (int j = 0; j < 4; ++j) acc[i][j] = mfma(wf[c & 1][j], __builtin_bit_cast(bf16x8, xa[c % (DA + 1)][i]), acc[i][j]);
    SB();
    if (c == 3 || c == 7 || c == 8) { wait_lgkm0(); bar(); }
  }
  s2_final_pre(p, fp, NRT, rt0, ctile, cw * 64 + lane);
  bar();
  constexpr u32 PR = 272, PH = NRT * 16 * PR;
#pragma unroll
  for (int i = 0; i < NRT; ++i)
#pragma unroll
    for (int j = 0; j < 4; ++j) lds_st_f4(cw * PH + (i * 16 + j16) * PR + (j * 16 + 4 * g4) * 4, acc[i][j]);
  wait_lgkm0();
}
__device__ __forceinline__ void s2_final(const Params& p, const FinPre& f, int nrt, int rt0, int ctile, int tid) {
  const u32 PR = 272, PH = nrt * 16 * PR;
  auto rs = __builtin_amdgcn_make_buffer_rsrc(p.out, 0, S * D * 2, 0x00020000);
  const int c8 = tid & 7;
#pragma unroll
  for (int it = 0; it < 2; ++it) {
    int r = (tid >> 3) + it * 64;
    if (r < nrt * 16) {
      u32 a = (u32)(r * PR + c8 * 32);
      f32x4 p0 = lds_f4(a), p1 = lds_f4(a + 16), q0 = lds_f4(a + PH), q1 = lds_f4(a + PH + 16);
      f32x4 r0 = lds_f4(a + 2 * PH), r1 = lds_f4(a + 2 * PH + 16), s0 = lds_f4(a + 3 * PH), s1 = lds_f4(a + 3 * PH + 16);
      float v[8];
#pragma unroll
      for (int e = 0; e < 4; ++e) { v[e] = (p0[e] + q0[e]) + (r0[e] + s0[e]); v[4 + e] = (p1[e] + q1[e]) + (r1[e] + s1[e]); }
#pragma unroll
      for (int e = 0; e < 4; ++e) { v[2 * e] += bf_lo(f.xv[it][e]) + bf_lo(f.bv[e]); v[2 * e + 1] += bf_hi(f.xv[it][e]) + bf_hi(f.bv[e]); }
      u32x4 o = {pack_bf16(v[0], v[1]), pack_bf16(v[2], v[3]), pack_bf16(v[4], v[5]), pack_bf16(v[6], v[7])};
      __builtin_amdgcn_raw_buffer_store_b128(o, rs, (u32)(((rt0 * 16 + r) * D + ctile * 64 + c8 * 8) * 2), 0, OUT_AUX);
    }
  }
}
__device__ __forceinline__ void publish(const Params& p, const Ids& id, u32 tag, int cw, int lane) {
  wait_vm<0>();
  if (lane == 0) st_agent(p.ws + WS_SYNC + id.sg * 8192 + SY_FLAG + (id.wgi * 4 + cw) * 4, tag);
}
__device__ __forceinline__ void csync() {
  wait_lgkm0();
  LDS_AS u32* cnt = (LDS_AS u32*)(uintptr_t)L_CNT;
  if ((threadIdx.x & 63) == 0) __hip_atomic_fetch_add(cnt, 1u, __ATOMIC_RELAXED, __HIP_MEMORY_SCOPE_WORKGROUP);
  for (int spin = 0; spin < SPIN_MAX; ++spin) {
    u32 c = __builtin_amdgcn_readfirstlane(__hip_atomic_load(cnt, __ATOMIC_RELAXED, __HIP_MEMORY_SCOPE_WORKGROUP));
    if (c >= 4u) break;
  }
  asm volatile("" ::: "memory");
}

__global__ __launch_bounds__(512, 2) void fused_kernel(Params p) {
  extern __shared__ __attribute__((aligned(16))) char smem[];
  const int tid = threadIdx.x, lane = tid & 63, wid = __builtin_amdgcn_readfirstlane(tid >> 6);
  Ids id;
  {
    const int b = blockIdx.x, xcd = b & 7, slot = b >> 3;
    const int xr = xcd >> 1, ml = slot >> 3, r8 = slot & 7;
    id.xc = xcd & 1; id.isw = r8 & 1; id.gi = r8 >> 1; id.g = 4 * id.xc + id.gi;
    id.mt = 4 * xr + ml; id.sg = id.mt >> 1; id.m2 = id.mt & 1;
    id.wgi = id.m2 * 16 + (id.isw ? 8 : 0) + id.g;
    id.rot1 = ((ml * 5 >> 1) + r8 + xr) % 10;
    id.roth = (ml * 3 + r8 + (r8 >> 1) + xr) % 12;
    id.roto = (ml + r8 + xr) % 5;
  }
  int rt0, ctile; u32 mtmask;
  if (id.isw) { ctile = 5 * id.xc + id.gi; rt0 = id.sg * 16 + (id.m2 ? 10 : 0); mtmask = id.m2 ? 2u : 1u; }
  else if (id.m2 == 0) { ctile = 5 * id.xc + id.gi; rt0 = id.sg * 16 + 6; mtmask = 3u; }
  else { ctile = 5 * id.xc + 4; rt0 = id.sg * 16 + 4 * id.gi; mtmask = (id.gi >> 1) ? 2u : 1u; }

  const bool isL = wid >= 4;
  const int w4 = wid & 3, mh = w4 >> 1, nh = w4 & 1;
  const int nrt = id.isw ? 6 : 4;
  FinPre fp;
  if (__builtin_expect(isL, 1)) {
    if (id.isw) s1_loader<true>(p, id, w4, lane); else s1_loader<false>(p, id, w4, lane);
    const u32 epoch = __builtin_amdgcn_readfirstlane(ld_agent(p.ws + WS_SYNC + id.sg * 8192 + SY_EPOCH));
    const u32 tag = (epoch + 1u) * 2654435761u;
    if (id.isw) consume_stats<true>(p, id, w4, lane, tag); else consume_stats<false>(p, id, w4, lane, tag);
    wait_lgkm0();
    bar();
    s2_loader(p, id, ctile, mtmask, tag, w4, lane);
    if (id.wgi == 0 && w4 == 0 && lane == 0) st_agent(p.ws + WS_SYNC + id.sg * 8192 + SY_EPOCH, epoch + 1u);
    s2_final_pre(p, fp, nrt, rt0, ctile, tid);
    bar();
  } else {
    if (w4 == 0 && lane < 25) {
      u32 val = lane < 24 ? 0x3f803f80u : 0u;
      u32 addr = lane < 24 ? (lane >> 3) * QA_SLOT + (lane & 7) * 16 : L_CNT;
      if (!id.isw || lane == 24) *(LDS_AS u32x4*)(uintptr_t)addr = u32x4{val, val, val, val};
    }
    const u32 tag = produce_stats(p, id, w4, lane);
    wait_lgkm0();
    if (id.isw) {
      f32x4 acc[4][6] = {};
      f32x4 acch[4] = {};
      const int cA = 6 * nh + 2 * mh, cB = 6 * nh + 4 - 4 * mh;
      s1_compute<2>(p, acc, acch, mh, lane, cA, cB, 0);
      store_colsum<2>(acch, lane, cA, cB, 0);
      wait_lgkm0();
      bar();
      gelu_store(p, id, acc, mh, cA, cB, lane);
    } else {
      if (nh == 0) {
        f32x4 acc[4][8] = {};
        f32x4 acch[2] = {};
        const int cA = 4 * mh, cB = 4 - 4 * mh;
        s1_compute<0>(p, acc, acch, mh, lane, cA, cB, 0);
        store_colsum<0>(acch, lane, cA, cB, 0);
        wait_lgkm0();
        bar();
        qkv_to_lds<0>(acc, acch, mh, lane, cA, cB, 0);
      } else {
        f32x4 acc[4][7] = {};
        f32x4 acch[6] = {};
        const int cA = mh ? 11 : 8, cB = mh ? 8 : 12, cX = 2 + 4 * mh;
        s1_compute<1>(p, acc, acch, mh, lane, cA, cB, cX);
        store_colsum<1>(acch, lane, cA, cB, cX);
        wait_lgkm0();
        bar();
        qkv_to_lds<1>(acc, acch, mh, lane, cA, cB, cX);
      }
      csync();
#pragma unroll 1
      for (int r = 0; r < 2; ++r) { attention(p, id.mt, id.g, 2 * w4 + r, lane); }
    }
    publish(p, id, tag, w4, lane);
    wait_lgkm0();
    if (id.isw) s2_compute_all<6>(p, id, rt0, w4, lane, mtmask, tag, fp, ctile);
    else s2_compute_all<4>(p, id, rt0, w4, lane, mtmask, tag, fp, ctile);
  }
  bar();
  s2_final(p, fp, nrt, rt0, ctile, tid);
}

extern "C" void kernel_launch(void* const* d_in, const int* in_sizes, int n_in, void* d_out, int out_size, void* d_ws, size_t ws_size,
                              hipStream_t stream) {
  Params p;
  p.x = (const u16*)d_in[0]; p.wqkv = (const u16*)d_in[1]; p.wo = (const u16*)d_in[2];
  p.w1 = (const u16*)d_in[4]; p.b1 = (const u16*)d_in[5]; p.w2 = (const u16*)d_in[6]; p.b2 = (const u16*)d_in[7];
  p.out = (u16*)d_out; p.ws = (char*)d_ws;
  (void)hipFuncSetAttribute((const void*)fused_kernel, hipFuncAttributeMaxDynamicSharedMemorySize, L_TOTAL);
  fused_kernel<<<256, 512, L_TOTAL, stream>>>(p);
}
```
